```python
import torch

def _softmax(x, axis=-1):
    return torch.softmax(x, dim=axis)

def _gelu(x):
    return torch.nn.functional.gelu(x, approximate='tanh')

def _silu(x):
    return torch.nn.functional.silu(x)

def _sigmoid(x):
    return torch.sigmoid(x)

def _layernorm(x, axis=-1, eps=1e-05):
    m = x.mean(dim=axis, keepdim=True)
    v = x.var(dim=axis, keepdim=True, unbiased=False)
    return (x - m) / torch.sqrt(v + eps)

def _scatter_add(tgt, idx, src, axis=0):
    return tgt.clone().index_add_(axis, idx, src)

def _segment_sum(src, seg, n):
    out = torch.zeros((n,) + src.shape[1:], dtype=src.dtype)
    return out.index_add_(0, seg, src)

def _pad(x, pw):
    flat = tuple((v for pair in reversed(pw) for v in pair))
    return torch.nn.functional.pad(x, flat)
kernel_name = 'standalone__window_attn_twins__vision'

def setup_inputs():

    _w = lambda a: (lambda t: t.to(torch.bfloat16) if t.is_floating_point() else t)(torch.as_tensor(a))
    H = W = 384 // 8 * 8
    C = 96
    return {'x': _w((lambda *s: torch.randn(*s, dtype=torch.bfloat16) * 0.8)(1, H, W, C)), 'wq': _w((lambda *s: torch.randn(*s, dtype=torch.bfloat16) * 0.8)(C, C)), 'wkv': _w((lambda *s: torch.randn(*s, dtype=torch.bfloat16) * 0.8)(C, 2 * C)), 'wo': _w((lambda *s: torch.randn(*s, dtype=torch.bfloat16) * 0.05)(C, C))}

def reference(x, wq, wkv, wo):
    B, H, W, C = x.shape
    win = 8
    nh, nw = (H // win, W // win)
    xs = x.reshape(B, nh, win, nw, win, C).mean(axis=(2, 4)).reshape(B, nh * nw, C)
    q = x.reshape(B, H * W, C) @ wq
    kv = xs @ wkv
    k, v = (kv[..., :C].clone(), kv[..., C:].clone())
    scale = C ** (-0.5)
    p = _softmax(torch.einsum('bqc,bkc->bqk', q, k) * scale, axis=-1)
    o = torch.einsum('bqk,bkc->bqc', p, v) @ wo
    return o.reshape(B, H, W, C)
```

```cpp
#include <hip/hip_runtime.h>
#include <hip/hip_bf16.h>
typedef unsigned short u16;
__device__ __forceinline__ float bf2f(u16 v){ return __uint_as_float(((unsigned)v)<<16); }
__device__ __forceinline__ u16 f2bf(float f){ unsigned u=__float_as_uint(f); u += 0x7fffu + ((u>>16)&1u); return (u16)(u>>16); }
#define HH 384
#define WW 384
#define CC 96
#define NKV 2304
#define NQ (HH*WW)
__global__ void pool_k(const u16* x, float* xs){
  int n = blockIdx.x; int c = threadIdx.x; if(c>=CC) return;
  int wh=n/48, ww=n%48; float s=0;
  for(int i=0;i<8;i++) for(int j=0;j<8;j++) s += bf2f(x[((size_t)(wh*8+i)*WW + (ww*8+j))*CC + c]);
  xs[n*CC+c] = bf2f(f2bf(s*(1.f/64.f)));
}
__global__ void kv_k(const float* xs, const u16* wkv, float* kv){
  int n=blockIdx.x; int c=threadIdx.x; float s=0;
  for(int k=0;k<CC;k++) s += xs[n*CC+k]*bf2f(wkv[k*192+c]);
  kv[n*192+c]=bf2f(f2bf(s));
}
__global__ void proj_k(const u16* in, const u16* w, u16* out){
  int q=blockIdx.x; int c=threadIdx.x; if(c>=CC) return; float s=0;
  for(int k=0;k<CC;k++) s += bf2f(in[(size_t)q*CC+k])*bf2f(w[k*CC+c]);
  out[(size_t)q*CC+c]=f2bf(s);
}
__global__ __launch_bounds__(64) void attn_k(const u16* qq, const float* kv, u16* o_out){
  int q = blockIdx.x*blockDim.x+threadIdx.x;
  __shared__ float sk[32*192];
  float qv[CC]; float o[CC];
#pragma unroll
  for(int c=0;c<CC;c++){ qv[c]=bf2f(qq[(size_t)q*CC+c]); o[c]=0; }
  float m=-1e30f,l=0;
  const float scale=0.10206207261596575f;
  for(int t=0;t<NKV;t+=32){
    __syncthreads();
    for(int i=threadIdx.x;i<32*192;i+=blockDim.x) sk[i]=kv[t*192+i];
    __syncthreads();
    for(int j=0;j<32;j++){
      float s=0;
#pragma unroll
      for(int c=0;c<CC;c++) s+=qv[c]*sk[j*192+c];
      s*=scale;
      float mn=fmaxf(m,s); float a=__expf(m-mn); float p=__expf(s-mn);
      l=l*a+p;
#pragma unroll
      for(int c=0;c<CC;c++) o[c]=o[c]*a+p*sk[j*192+96+c];
      m=mn;
    }
  }
  float il=1.f/l;
#pragma unroll
  for(int c=0;c<CC;c++) o_out[(size_t)q*CC+c]=f2bf(o[c]*il);
}
extern "C" void kernel_launch(void* const* d_in, const int* in_sizes, int n_in, void* d_out, int out_size, void* d_ws, size_t ws_size, hipStream_t stream){
  const u16* x=(const u16*)d_in[0]; const u16* wq=(const u16*)d_in[1]; const u16* wkv=(const u16*)d_in[2]; const u16* wo=(const u16*)d_in[3];
  float* xs=(float*)d_ws; float* kv=xs+NKV*CC; u16* q=(u16*)(kv+NKV*192); u16* o=q+(size_t)NQ*CC;
  pool_k<<<NKV,128,0,stream>>>(x,xs);
  kv_k<<<NKV,192,0,stream>>>(xs,wkv,kv);
  proj_k<<<NQ,128,0,stream>>>(x,wq,q);
  attn_k<<<NQ/64,64,0,stream>>>(q,kv,o);
  proj_k<<<NQ,128,0,stream>>>(o,wo,(u16*)d_out);
}
```

```cpp
#include <hip/hip_runtime.h>
#include <utility>

typedef unsigned short u16;
typedef float    f32x16 __attribute__((ext_vector_type(16)));
typedef unsigned u32x4  __attribute__((ext_vector_type(4)));
typedef unsigned u32x2  __attribute__((ext_vector_type(2)));
template <int... Is> using iseq = std::integer_sequence<int, Is...>;
template <int N> using make_iseq = std::make_integer_sequence<int, N>;
#define DI __device__ __forceinline__
#define GAP() __builtin_amdgcn_sched_barrier(0)
#define ACLOB "a0","a1","a2","a3","a4","a5","a6","a7","a8","a9","a10","a11","a12","a13","a14","a15","a16","a17","a18","a19","a20","a21","a22","a23","a24","a25","a26","a27","a28","a29","a30","a31","a32","a33","a34","a35","a36","a37","a38","a39","a40","a41","a42","a43","a44","a45","a46","a47","a48","a49","a50","a51","a52","a53","a54","a55","a56","a57","a58","a59","a60","a61","a62","a63","a64","a65","a66","a67","a68","a69","a70","a71","a72","a73","a74","a75","a76","a77","a78","a79","a80","a81","a82","a83","a84","a85","a86","a87","a88","a89","a90","a91","a92","a93","a94","a95","a96","a97","a98","a99","a100","a101","a102","a103","a104","a105","a106","a107","a108","a109","a110","a111","a112","a113","a114","a115","a116","a117","a118","a119","a120","a121","a122","a123","a124","a125","a126","a127","a128","a129","a130","a131","a132","a133","a134","a135","a136","a137","a138","a139","a140","a141","a142","a143","a144","a145","a146","a147","a148","a149","a150","a151","a152","a153","a154","a155","a156","a157","a158","a159","a160","a161","a162","a163","a164","a165","a166","a167","a168","a169","a170","a171","a172","a173","a174","a175","a176","a177","a178","a179","a180","a181","a182","a183","a184","a185","a186","a187","a188","a189","a190","a191","a192","a193","a194","a195","a196","a197","a198","a199","a200","a201","a202","a203","a204","a205","a206","a207","a208","a209","a210","a211","a212","a213","a214","a215","a216","a217","a218","a219","a220","a221","a222","a223","a224","a225","a226","a227","a228","a229","a230","a231","a232","a233","a234","a235","a236","a237","a238","a239","a240","a241","a242","a243","a244","a245","a246","a247","a248","a249","a250","a251","a252","a253","a254","a255"

constexpr int IMG_W = 384, CD = 96, NT = 36;
constexpr unsigned TILEB = 24576;
constexpr size_t WS_WQ = (size_t)NT * TILEB, WS_WO = WS_WQ + 18432, WS_END = WS_WO + 18432;
constexpr float  CSCALE = 0.10206207261596575f * 1.4426950408889634f;
#define THRV 20.0f
constexpr float  THR = THRV;
constexpr unsigned KRING = 0, VRING = 24576, WQL = 49152, WOL = 67584, OSTG = 86016, OSTG_W = 13312, LDS_BYTES = OSTG + 4 * OSTG_W;

DI float bf2f(u16 v) { return __uint_as_float(((unsigned)v) << 16); }
DI u16 f2bf(float f) { unsigned u = __float_as_uint(f); u += 0x7fffu + ((u >> 16) & 1u); return (u16)(u >> 16); }
DI unsigned cvtpk(float lo, float hi) { unsigned r; asm("v_cvt_pk_bf16_f32 %0, %1, %2" : "=v"(r) : "v"(lo), "v"(hi)); return r; }
DI float max3f(float a, float b, float c) { float r; asm("v_max3_f32 %0, %1, %2, %3" : "=v"(r) : "v"(a), "v"(b), "v"(c)); return r; }
DI float max2f(float a, float b) { float r; asm("v_max_f32_e32 %0, %1, %2" : "=v"(r) : "v"(a), "v"(b)); return r; }
DI float fadd_s(float a, float b) { float r; asm("v_add_f32_e32 %0, %1, %2" : "=v"(r) : "v"(a), "v"(b)); return r; }
DI float fmul_s(float a, float b) { float r; asm("v_mul_f32_e32 %0, %1, %2" : "=v"(r) : "v"(a), "v"(b)); return r; }
DI float fsub_s(float a, float b) { float r; asm("v_sub_f32_e32 %0, %1, %2" : "=v"(r) : "v"(a), "v"(b)); return r; }
DI unsigned rfl(unsigned x) { return __builtin_amdgcn_readfirstlane(x); }

__global__ __launch_bounds__(192) void prep_kernel(const u16* __restrict__ x, const u16* __restrict__ wq, const u16* __restrict__ wkv,
                                                   const u16* __restrict__ wo, char* __restrict__ ws) {
  __shared__ float sums[16][96];
  __shared__ float xs[2][96];
  const int t = threadIdx.x;
  if (blockIdx.x >= 1152) {
    const bool isq = blockIdx.x == 1152;
    u16* dst = (u16*)(ws + (isq ? WS_WQ : WS_WO));
    const u16* w = isq ? wq : wo;
    for (int i = t; i < 9216; i += 192) {
      const int j = i & 7, lane = (i >> 3) & 63, f = i >> 9, b = f / 6, s = f % 6, r = lane & 31, h = lane >> 5;
      int d;
      if (isq) d = 16 * s + 8 * h + j;
      else     d = 32 * (s >> 1) + 16 * (s & 1) + 8 * (j >> 2) + 4 * h + (j & 3);
      dst[i] = w[d * 96 + 32 * b + r];
    }
    return;
  }
  const int wh = blockIdx.x / 24, p = blockIdx.x % 24;
  const int ps = t / 12, g = t % 12;
  float a[8];
#pragma unroll
  for (int e = 0; e < 8; ++e) a[e] = 0.f;
  u32x4 v[8];
#pragma unroll
  for (int i = 0; i < 8; ++i) v[i] = *(const u32x4*)(x + ((size_t)(wh * 8 + i) * IMG_W + 16 * p + ps) * CD + 8 * g);
#pragma unroll
  for (int i = 0; i < 8; ++i)
#pragma unroll
    for (int e = 0; e < 4; ++e) { a[2 * e] += __uint_as_float(v[i][e] << 16); a[2 * e + 1] += __uint_as_float(v[i][e] & 0xffff0000u); }
#pragma unroll
  for (int e = 0; e < 8; ++e) sums[ps][8 * g + e] = a[e];
  __syncthreads();
  { const int wsel = t / 96, c = t % 96; float s = 0.f;
#pragma unroll
    for (int j = 0; j < 8; ++j) s += sums[8 * wsel + j][c];
    xs[wsel][c] = bf2f(f2bf(s * (1.f / 64.f))); }
  __syncthreads();
  float acc0 = 0.f, acc1 = 0.f;
#pragma unroll 8
  for (int k = 0; k < 96; ++k) { const float w = bf2f(wkv[k * 192 + t]); acc0 = fmaf(xs[0][k], w, acc0); acc1 = fmaf(xs[1][k], w, acc1); }
  u16* img = (u16*)ws;
#pragma unroll
  for (int wsel = 0; wsel < 2; ++wsel) {
    const float val = wsel ? acc1 : acc0;
    const int n = wh * 48 + 2 * p + wsel, tile = n >> 6, kk = n & 63;
    if (t < 96) {
      const int d = t, b = d >> 5, s = (d >> 4) & 1, jhi = (d >> 3) & 1, h = (d >> 2) & 1, jlo = d & 3;
      const int kb = kk >> 5, r = kk & 31, f = kb * 6 + 2 * b + s, lane = 32 * h + r;
      img[(size_t)tile * (TILEB / 2) + (f * 64 + lane) * 8 + 4 * jhi + jlo] = f2bf(bf2f(f2bf(val)) * CSCALE);
    } else {
      const int d = t - 96, db = d >> 5, r = d & 31;
      const int kb = kk >> 5, s = (kk >> 4) & 1, jhi = (kk >> 3) & 1, h = (kk >> 2) & 1, jlo = kk & 3;
      const int f = db * 4 + 2 * kb + s, lane = 32 * h + r;
      img[(size_t)tile * (TILEB / 2) + 6144 + (f * 64 + lane) * 8 + 4 * jhi + jlo] = f2bf(val);
    }
  }
}

constexpr int AO_(int db, int qb) { return 16 * (2 * db + qb); }
constexpr int AQ_(int qb, int d0) { return 96 + 24 * qb + 4 * d0; }
constexpr int AK_(int kb, int d0) { return 144 + 24 * kb + 4 * d0; }
constexpr int AV_(int db, int ks) { return 192 + 16 * db + 4 * ks; }

#define STAMP(c) do {} while (0)
struct Ctx {
  int nst;
  unsigned vl16, voff, vw;
  __amdgpu_buffer_rsrc_t srd;
  unsigned m0k[2], m0v[2], swv;
  unsigned sk, sv;
  f32x16 S[2][2][2];
  f32x16 NEGM[2];
  u32x4  Pk[2][4];
  float mhat[2], lsum[2], fct[2]; bool pend;
  float rs0[2], rs1[2], pm0[2], pm1[2], rmax[2];
};

DI float w_aone() { unsigned l; asm volatile("v_mbcnt_lo_u32_b32 %0, -1, 0\n\tv_mbcnt_hi_u32_b32 %0, -1, %0" : "=v"(l)); return l < 32u ? 1.f : 0.f; }
DI void negm_bcast(f32x16& negm, float aone, float neg_mhat) {
  asm volatile("s_nop 1\n\tv_mfma_f32_32x32x2_f32 %0, %1, %2, 0\n\ts_nop 7\n\ts_nop 7\n\ts_nop 3" : "+v"(negm) : "v"(aone), "v"(neg_mhat));
}
template <int I, int SLOT> DI void k_read(Ctx& c) {
  constexpr int kb = I / 6, d0 = I % 6;
  asm volatile("ds_read_b128 a[%c1:%c2], %0 offset:%c3" :: "v"(c.vl16), "i"(AK_(kb, d0)), "i"(AK_(kb, d0) + 3), "i"(KRING + 12288 * SLOT + 1024 * I) : "memory", ACLOB);
}
template <int I, int SLOT> DI void v_read(Ctx& c) {
  constexpr int db = I / 4, ks = I % 4;
  asm volatile("ds_read_b128 a[%c1:%c2], %0 offset:%c3" :: "v"(c.vl16), "i"(AV_(db, ks)), "i"(AV_(db, ks) + 3), "i"(VRING + 12288 * SLOT + 1024 * I) : "memory", ACLOB);
}
template <int NQB, int X, bool FIRST, int M> DI void qk_mfma(Ctx& c) {
  constexpr int d0 = M / (2 * NQB), kb = (M / NQB) % 2, qb = M % NQB, ak = AK_(kb, d0), aq = AQ_(qb, d0);
  if constexpr (d0 == 0) {
    if constexpr (FIRST) asm volatile("v_mfma_f32_32x32x16_bf16 %0, a[%c1:%c2], a[%c3:%c4], 0" : "=&v"(c.S[X][kb][qb]) : "i"(ak), "i"(ak + 3), "i"(aq), "i"(aq + 3) : ACLOB);
    else                 asm volatile("v_mfma_f32_32x32x16_bf16 %0, a[%c2:%c3], a[%c4:%c5], %1" : "=&v"(c.S[X][kb][qb]) : "v"(c.NEGM[qb]), "i"(ak), "i"(ak + 3), "i"(aq), "i"(aq + 3) : ACLOB);
  } else                 asm volatile("v_mfma_f32_32x32x16_bf16 %0, a[%c1:%c2], a[%c3:%c4], %0" : "+v"(c.S[X][kb][qb]) : "i"(ak), "i"(ak + 3), "i"(aq), "i"(aq + 3) : ACLOB);
}
template <int NQB, int M, bool ZERO> DI void pv_mfma(Ctx& c) {
  constexpr int ks = M / (3 * NQB), db = (M / NQB) % 3, qb = M % NQB, ao = AO_(db, qb), av = AV_(db, ks);
  if constexpr (ZERO && ks == 0) asm volatile("v_mfma_f32_32x32x16_bf16 a[%c1:%c2], a[%c3:%c4], %0, 0" :: "v"(c.Pk[qb][ks]), "i"(ao), "i"(ao + 15), "i"(av), "i"(av + 3) : ACLOB);
  else                           asm volatile("v_mfma_f32_32x32x16_bf16 a[%c1:%c2], a[%c3:%c4], %0, a[%c1:%c2]" :: "v"(c.Pk[qb][ks]), "i"(ao), "i"(ao + 15), "i"(av), "i"(av + 3) : ACLOB);
}
template <int P> DI void dma_piece(Ctx& c, unsigned m0v, unsigned soff) {
  asm volatile("s_mov_b32 m0, %0\n\ts_nop 0\n\tbuffer_load_dwordx4 %1, %2, %3 offen offset:%c4 lds" :: "s"(m0v), "v"(c.voff), "s"(c.srd), "s"(soff), "i"(1024 * P) : "memory");
}
template <int X, int QB, int KS, int P> DI void pack1(Ctx& c) {
  c.Pk[QB][KS][P] = cvtpk(c.S[X][KS >> 1][QB][8 * (KS & 1) + 2 * P], c.S[X][KS >> 1][QB][8 * (KS & 1) + 2 * P + 1]);
}
template <int X, int KB, int E> DI void exp1(Ctx& c) {
 c.S[X][KB][E >> 4][E & 15] = __builtin_amdgcn_exp2f(c.S[X][KB][E >> 4][E & 15]); }
template <int X, int KB, int E> DI void sum1(Ctx& c) {
  if constexpr (E & 1) c.rs1[E >> 4] = fadd_s(c.rs1[E >> 4], c.S[X][KB][E >> 4][E & 15]);
  else                 c.rs0[E >> 4] = fadd_s(c.rs0[E >> 4], c.S[X][KB][E >> 4][E & 15]);
}

template <int NQB, int X, bool FIRST, int M> DI void gapA(Ctx& c) {
  constexpr int Xp = X ^ 1;
  qk_mfma<NQB, X, FIRST, M>(c);
  if constexpr (!FIRST) {
    if constexpr (M < 12) v_read<M, Xp>(c);
    if constexpr (M < 8 * NQB) {
      exp1<Xp, 1, 2 * M>(c); exp1<Xp, 1, 2 * M + 1>(c);
      pack1<Xp, (M >> 3), ((M >> 2) & 1), (M & 3)>(c);
    } else {
      constexpr int g = M - 8 * NQB, i0 = 2 * g, i1 = 2 * g + 1;
      pack1<Xp, (i0 >> 3), 2 + ((i0 >> 2) & 1), (i0 & 3)>(c);
      pack1<Xp, (i1 >> 3), 2 + ((i1 >> 2) & 1), (i1 & 3)>(c);
      sum1<Xp, 1, 16 * (i0 >> 3) + 8 * ((i0 >> 2) & 1) + 2 * (i0 & 3)>(c); sum1<Xp, 1, 16 * (i0 >> 3) + 8 * ((i0 >> 2) & 1) + 2 * (i0 & 3) + 1>(c);
      sum1<Xp, 1, 16 * (i1 >> 3) + 8 * ((i1 >> 2) & 1) + 2 * (i1 & 3)>(c); sum1<Xp, 1, 16 * (i1 >> 3) + 8 * ((i1 >> 2) & 1) + 2 * (i1 & 3) + 1>(c);
    }
  }
  GAP();
}
template <int NQB, int X, bool FIRST, int... Ms> DI void phaseA_(Ctx& c, iseq<Ms...>) {
  (gapA<NQB, X, FIRST, Ms>(c), ...);
  if constexpr (!FIRST) {
#pragma unroll
    for (int q = 0; q < NQB; ++q) {
      const float rs = fadd_s(c.rs0[q], c.rs1[q]);
      auto pr = __builtin_amdgcn_permlane32_swap(__float_as_uint(rs), __float_as_uint(rs), false, false);
      c.lsum[q] = fadd_s(c.lsum[q], fadd_s(__uint_as_float(pr[0]), __uint_as_float(pr[1])));
    }
  }
  asm volatile("s_waitcnt vmcnt(0) lgkmcnt(0)\n\ts_barrier" ::: "memory");
  GAP();
}
template <int NQB, int X, bool FIRST> DI void phaseA(Ctx& c) { phaseA_<NQB, X, FIRST>(c, make_iseq<12 * NQB>{}); }

template <int NQB, int X, int I> DI void max1(Ctx& c) {
  constexpr int q = I >> 4, k = I & 15;
  if constexpr (k == 0)      c.pm0[q] = max2f(c.S[X][0][q][0], c.S[X][1][q][0]);
  else if constexpr (k == 1) c.pm1[q] = max2f(c.S[X][0][q][1], c.S[X][1][q][1]);
  else if constexpr (k & 1)  c.pm1[q] = max3f(c.pm1[q], c.S[X][0][q][k], c.S[X][1][q][k]);
  else                       c.pm0[q] = max3f(c.pm0[q], c.S[X][0][q][k], c.S[X][1][q][k]);
}
template <int NQB, int X, int LO, int HI> DI void max_range(Ctx& c) { if constexpr (LO < HI) { max1<NQB, X, LO>(c); max_range<NQB, X, LO + 1, HI>(c); } }
template <int X, int LO, int HI> DI void exp_range(Ctx& c) { if constexpr (LO < HI) { exp1<X, 0, LO>(c); exp_range<X, LO + 1, HI>(c); } }
template <int X, int KB, int LO, int HI> DI void sum_range(Ctx& c) { if constexpr (LO < HI) { sum1<X, KB, LO>(c); sum_range<X, KB, LO + 1, HI>(c); } }

template <int NQB, int X, bool PEEL, bool ZERO1, bool DK, bool DV, bool KR, int M> DI void gapB(Ctx& c) {
  constexpr int GB = 12 * NQB, gM = 5 * NQB, NE = 16 * NQB, NG = GB - gM - 1;
  if constexpr (!PEEL) pv_mfma<NQB, M, ZERO1>(c);
  if constexpr (M < 3 && DK) dma_piece<M>(c, c.m0k[X], c.sk);
  if constexpr (M >= 3 && M < 6 && DV) dma_piece<M - 3>(c, c.m0v[X ^ 1], c.sv);
  if constexpr (KR && M < 12) k_read<M, X ^ 1>(c);
  if constexpr (M < gM) max_range<NQB, X, (NE * M) / gM, (NE * (M + 1)) / gM>(c);
  if constexpr (M == gM) {
#pragma unroll
    for (int q = 0; q < NQB; ++q) {
      const float mm = max2f(c.pm0[q], c.pm1[q]);
      auto pr = __builtin_amdgcn_permlane32_swap(__float_as_uint(mm), __float_as_uint(mm), false, false);
      c.rmax[q] = max2f(__uint_as_float(pr[0]), __uint_as_float(pr[1]));
    }
    if constexpr (PEEL) {
#pragma unroll
      for (int q = 0; q < NQB; ++q) {
        c.mhat[q] = c.rmax[q]; c.lsum[q] = 0.f; c.fct[q] = 1.f;
#pragma unroll
        for (int kb = 0; kb < 2; ++kb)
#pragma unroll
          for (int e = 0; e < 16; ++e) c.S[X][kb][q][e] = fsub_s(c.S[X][kb][q][e], c.rmax[q]);
        negm_bcast(c.NEGM[q], w_aone(), -c.mhat[q]);
      }
    } else {
      bool grew = c.rmax[0] > THR;
      if constexpr (NQB == 2) grew = grew | (c.rmax[1] > THR);
      if (__builtin_expect(__any((int)grew), 0)) {
#pragma unroll
        for (int q = 0; q < NQB; ++q) {
          const float dl = max2f(c.rmax[q], 0.f);
          c.mhat[q] = fadd_s(c.mhat[q], dl); c.fct[q] = __builtin_amdgcn_exp2f(-dl);
#pragma unroll
          for (int kb = 0; kb < 2; ++kb)
#pragma unroll
            for (int e = 0; e < 16; ++e) c.S[X][kb][q][e] = fsub_s(c.S[X][kb][q][e], dl);
          negm_bcast(c.NEGM[q], w_aone(), -c.mhat[q]);
        }
        c.pend = true;
      }
    }
#pragma unroll
    for (int q = 0; q < NQB; ++q) { c.rs0[q] = 0.f; c.rs1[q] = 0.f; }
  }
  if constexpr (M > gM) {
    constexpr int g = M - gM - 1, lo = (NE * g) / NG, hi = (NE * (g + 1)) / NG;
    if constexpr (g > 0) sum_range<X, 0, (NE * (g - 1)) / NG, lo>(c);
    exp_range<X, lo, hi>(c);
  }
  GAP();
}
template <int A> DI void resc1(float f0, float f1) {
  float t; asm volatile("v_accvgpr_read_b32 %0, a[%c1]" : "=v"(t) : "i"(A) : ACLOB);
  t = fmul_s(t, ((A >> 4) & 1) ? f1 : f0);
  asm volatile("v_accvgpr_write_b32 a[%c1], %0" :: "v"(t), "i"(A) : ACLOB);
}
template <int... As> DI void resc_all(float f0, float f1, iseq<As...>) { (resc1<As>(f0, f1), ...); }
template <int NQB, int X, bool PEEL, bool ZERO1, bool DK, bool DV, bool KR, int... Ms> DI void phaseB_(Ctx& c, iseq<Ms...>) {
  constexpr int GB = 12 * NQB, gM = 5 * NQB, NE = 16 * NQB, NG = GB - gM - 1;
  (gapB<NQB, X, PEEL, ZERO1, DK, DV, KR, Ms>(c), ...);
  sum_range<X, 0, (NE * (NG - 1)) / NG, NE>(c);
  if constexpr (!PEEL) {
    if (__builtin_expect(c.pend, 0)) {
      asm volatile("s_nop 7\n\ts_nop 7" ::: "memory");
      resc_all(c.fct[0], c.fct[NQB - 1], make_iseq<96>{});
      asm volatile("s_nop 1" ::: "memory");
#pragma unroll
      for (int q = 0; q < NQB; ++q) c.lsum[q] = fmul_s(c.lsum[q], c.fct[q]);
      c.pend = false;
    }
  }
  asm volatile("s_waitcnt lgkmcnt(0)" ::: "memory");
  GAP();
}
template <int NQB, int X, bool PEEL, bool ZERO1, bool DK = true, bool DV = true, bool KR = true> DI void phaseB(Ctx& c) {
  phaseB_<NQB, X, PEEL, ZERO1, DK, DV, KR>(c, make_iseq<12 * NQB>{});
}
template <int SLOT, int... Is> DI void k_reads(Ctx& c, iseq<Is...>) { (k_read<Is, SLOT>(c), ...); }
template <int SLOT, int... Is> DI void v_reads(Ctx& c, iseq<Is...>) { (v_read<Is, SLOT>(c), ...); }
template <int NQB, int... Ms> DI void pv_all(Ctx& c, iseq<Ms...>) { (pv_mfma<NQB, Ms, false>(c), ...); }
template <int A> DI float acc_read(void) { float t; asm volatile("v_accvgpr_read_b32 %0, a[%c1]" : "=v"(t) : "i"(A) : ACLOB); return t; }
template <int A> DI void acc_write(unsigned v) { asm volatile("v_accvgpr_write_b32 a[%c1], %0" :: "v"(v), "i"(A) : ACLOB); }

template <int NQB, int I> DI void q_to_acc1(f32x16 (&qa)[3][NQB]) {
  constexpr int qb = I / 24, d0 = (I / 4) % 6, i = I % 4, b = d0 / 2, s = d0 % 2;
  acc_write<AQ_(qb, d0) + i>(cvtpk(qa[b][qb][8 * s + 2 * i], qa[b][qb][8 * s + 2 * i + 1]));
}
template <int NQB, int... Is> DI void q_to_acc(f32x16 (&qa)[3][NQB], iseq<Is...>) { (q_to_acc1<NQB, Is>(qa), ...); }
template <int QB, int I> DI void o_to_frag1(u32x4 (&of)[6], float il) {
  constexpr int db = I / 8, i = I % 8;
  const float a = fmul_s(acc_read<AO_(db, QB) + 2 * i>(), il), b = fmul_s(acc_read<AO_(db, QB) + 2 * i + 1>(), il);
  of[2 * db + i / 4][i % 4] = cvtpk(a, b);
}
template <int QB, int... Is> DI void o_to_frag(u32x4 (&of)[6], float il, iseq<Is...>) { (o_to_frag1<QB, Is>(of, il), ...); }
template <int NQB> DI void set_tile_offsets(Ctx& c, int t) {
  c.sk = rfl((unsigned)(t + 2) * TILEB + c.swv);
  c.sv = rfl((unsigned)(t + 1) * TILEB + 12288u + c.swv);
}

template <int NQB> DI void run_block(Ctx& c, const u16* __restrict__ x, u16* __restrict__ out, unsigned row0, unsigned ldsb, unsigned wid, unsigned lane) {
  const unsigned r = lane & 31u, h = lane >> 5;
  STAMP(c);
  u32x4 xf[NQB][6];
#pragma unroll
  for (int qb = 0; qb < NQB; ++qb) {
    const unsigned row = row0 + (NQB == 2 ? 32u * qb + r : (r & 15u));
#pragma unroll
    for (int s = 0; s < 6; ++s) xf[qb][s] = *(const u32x4*)(x + (size_t)row * CD + 16 * s + 8 * h);
  }
  { const unsigned s0 = rfl(c.swv), s1 = rfl(TILEB + c.swv), sv0 = rfl(12288u + c.swv);
    dma_piece<0>(c, c.m0k[0], s0); dma_piece<1>(c, c.m0k[0], s0); dma_piece<2>(c, c.m0k[0], s0);
    dma_piece<0>(c, c.m0v[0], sv0); dma_piece<1>(c, c.m0v[0], sv0); dma_piece<2>(c, c.m0v[0], sv0);
    dma_piece<0>(c, c.m0k[1], s1); dma_piece<1>(c, c.m0k[1], s1); dma_piece<2>(c, c.m0k[1], s1); }
  f32x16 qa[3][NQB];
#pragma unroll
  for (int b = 0; b < 3; ++b) {
    u32x4 wf[6];
#pragma unroll
    for (int s = 0; s < 6; ++s) asm volatile("ds_read_b128 %0, %1 offset:%c2" : "=v"(wf[s]) : "v"(c.vw), "i"(1024 * (6 * b + s)) : "memory");
    asm volatile("s_waitcnt lgkmcnt(0)" : "+v"(wf[0]), "+v"(wf[1]), "+v"(wf[2]), "+v"(wf[3]), "+v"(wf[4]), "+v"(wf[5]) :: "memory");
#pragma unroll
    for (int s = 0; s < 6; ++s)
#pragma unroll
      for (int qb = 0; qb < NQB; ++qb) {
        if (s == 0) asm volatile("v_mfma_f32_32x32x16_bf16 %0, %1, %2, 0" : "=&v"(qa[b][qb]) : "v"(wf[s]), "v"(xf[qb][s]));
        else        asm volatile("v_mfma_f32_32x32x16_bf16 %0, %1, %2, %0" : "+v"(qa[b][qb]) : "v"(wf[s]), "v"(xf[qb][s]));
      }
  }
  asm volatile("s_nop 7\n\ts_nop 7" ::: "memory");
  q_to_acc<NQB>(qa, make_iseq<24 * NQB>{});
  asm volatile("s_waitcnt vmcnt(0)\n\ts_barrier" ::: "memory");
  k_reads<0>(c, make_iseq<12>{});
  asm volatile("s_waitcnt lgkmcnt(0)\n\ts_nop 1" ::: "memory");
  c.pend = false;

  STAMP(c);
  set_tile_offsets<NQB>(c, 0);
  phaseA<NQB, 0, true>(c);  phaseB<NQB, 0, true, false>(c);
  set_tile_offsets<NQB>(c, 1);
  phaseA<NQB, 1, false>(c); phaseB<NQB, 1, false, true>(c);
  for (int t = 2; t < NT - 2; t += 2) {
    set_tile_offsets<NQB>(c, t);
    phaseA<NQB, 0, false>(c); phaseB<NQB, 0, false, false>(c);
    set_tile_offsets<NQB>(c, t + 1);
    phaseA<NQB, 1, false>(c); phaseB<NQB, 1, false, false>(c);
  }
  set_tile_offsets<NQB>(c, NT - 2);
  phaseA<NQB, 0, false>(c); phaseB<NQB, 0, false, false, false, true, true>(c);
  phaseA<NQB, 1, false>(c); phaseB<NQB, 1, false, false, false, false, false>(c);
  STAMP(c);
  v_reads<1>(c, make_iseq<12>{});
#pragma unroll
  for (int q = 0; q < NQB; ++q)
#pragma unroll
    for (int e = 0; e < 16; ++e) c.S[1][1][q][e] = __builtin_amdgcn_exp2f(c.S[1][1][q][e]);
#pragma unroll
  for (int q = 0; q < NQB; ++q)
#pragma unroll
    for (int ks = 0; ks < 4; ++ks)
#pragma unroll
      for (int p = 0; p < 4; ++p) c.Pk[q][ks][p] = cvtpk(c.S[1][ks >> 1][q][8 * (ks & 1) + 2 * p], c.S[1][ks >> 1][q][8 * (ks & 1) + 2 * p + 1]);
#pragma unroll
  for (int q = 0; q < NQB; ++q) {
#pragma unroll
    for (int e = 0; e < 16; e += 2) { c.rs0[q] = fadd_s(c.rs0[q], c.S[1][1][q][e]); c.rs1[q] = fadd_s(c.rs1[q], c.S[1][1][q][e + 1]); }
    const float rs = fadd_s(c.rs0[q], c.rs1[q]);
    auto pr = __builtin_amdgcn_permlane32_swap(__float_as_uint(rs), __float_as_uint(rs), false, false);
    c.lsum[q] = fadd_s(c.lsum[q], fadd_s(__uint_as_float(pr[0]), __uint_as_float(pr[1])));
  }
  asm volatile("s_waitcnt lgkmcnt(0)\n\ts_nop 1" ::: "memory");
  GAP();
  pv_all<NQB>(c, make_iseq<12 * NQB>{});
  asm volatile("s_nop 7\n\ts_nop 7" ::: "memory");
  STAMP(c);
  u32x4 of[NQB][6];
  o_to_frag<0>(of[0], __builtin_amdgcn_rcpf(c.lsum[0]), make_iseq<24>{});
  if constexpr (NQB == 2) o_to_frag<1>(of[NQB - 1], __builtin_amdgcn_rcpf(c.lsum[NQB - 1]), make_iseq<24>{});
  asm volatile("s_nop 1" ::: "memory");
  f32x16 oa[3][NQB];
#pragma unroll
  for (int b = 0; b < 3; ++b) {
    u32x4 wf[6];
#pragma unroll
    for (int s = 0; s < 6; ++s) asm volatile("ds_read_b128 %0, %1 offset:%c2" : "=v"(wf[s]) : "v"(c.vw), "i"(18432 + 1024 * (6 * b + s)) : "memory");
    asm volatile("s_waitcnt lgkmcnt(0)" : "+v"(wf[0]), "+v"(wf[1]), "+v"(wf[2]), "+v"(wf[3]), "+v"(wf[4]), "+v"(wf[5]) :: "memory");
#pragma unroll
    for (int s = 0; s < 6; ++s)
#pragma unroll
      for (int qb = 0; qb < NQB; ++qb) {
        if (s == 0) asm volatile("v_mfma_f32_32x32x16_bf16 %0, %1, %2, 0" : "=&v"(oa[b][qb]) : "v"(wf[s]), "v"(of[qb][s]));
        else        asm volatile("v_mfma_f32_32x32x16_bf16 %0, %1, %2, %0" : "+v"(oa[b][qb]) : "v"(wf[s]), "v"(of[qb][s]));
      }
  }
  asm volatile("s_nop 7\n\ts_nop 7" ::: "memory");
  extern __shared__ __attribute__((aligned(16))) char lds_[];
  char* st = lds_ + OSTG + OSTG_W * wid;
#pragma unroll
  for (int qb = 0; qb < NQB; ++qb)
#pragma unroll
    for (int b = 0; b < 3; ++b)
#pragma unroll
      for (int g = 0; g < 4; ++g) {
        u32x2 w; w[0] = cvtpk(oa[b][qb][4 * g], oa[b][qb][4 * g + 1]); w[1] = cvtpk(oa[b][qb][4 * g + 2], oa[b][qb][4 * g + 3]);
        *(u32x2*)(st + (32u * qb + r) * 208u + 64u * b + 16u * g + 8u * h) = w;
      }
  asm volatile("s_waitcnt lgkmcnt(0)" ::: "memory");
  constexpr int NCH = NQB == 2 ? 12 : 3;
#pragma unroll
  for (int it = 0; it < NCH; ++it) {
    const unsigned idx = 64u * it + lane, rr = idx / 12u, ch = idx % 12u;
    const u32x4 v = *(const u32x4*)(st + rr * 208u + ch * 16u);
    *(u32x4*)(out + (size_t)row0 * CD + idx * 8u) = v;
  }
  asm volatile("s_waitcnt lgkmcnt(0)" ::: "memory");
  STAMP(c);
}

extern "C" __global__ __launch_bounds__(256, 1)
void attn_main(const u16* __restrict__ x, const char* __restrict__ ws, u16* __restrict__ out) {
  extern __shared__ __attribute__((aligned(16))) char lds[];
  const unsigned tid = threadIdx.x, lane = tid & 63u, wid = rfl(tid >> 6);
  const unsigned ldsb = (unsigned)(size_t)lds;
  Ctx c; c.nst = 0;
  c.vl16 = ldsb + 16u * lane; c.voff = 16u * lane; c.vw = ldsb + WQL + 16u * lane;
  c.srd = __builtin_amdgcn_make_buffer_rsrc((void*)ws, (short)0, (int)WS_END, 0x00020000);
  c.swv = rfl(3072u * wid);
  c.m0k[0] = rfl(ldsb + KRING + 3072u * wid); c.m0k[1] = rfl(ldsb + KRING + 12288u + 3072u * wid);
  c.m0v[0] = rfl(ldsb + VRING + 3072u * wid); c.m0v[1] = rfl(ldsb + VRING + 12288u + 3072u * wid);
  { const unsigned m0w = rfl(ldsb + WQL + 9216u * wid), sw = rfl((unsigned)WS_WQ + 9216u * wid);
    dma_piece<0>(c, m0w, sw); dma_piece<1>(c, m0w, sw); dma_piece<2>(c, m0w, sw); dma_piece<3>(c, m0w, sw);
    const unsigned m0w2 = rfl(m0w + 4096u), sw2 = rfl(sw + 4096u);
    dma_piece<0>(c, m0w2, sw2); dma_piece<1>(c, m0w2, sw2); dma_piece<2>(c, m0w2, sw2); dma_piece<3>(c, m0w2, sw2);
    const unsigned m0w3 = rfl(m0w + 8192u), sw3 = rfl(sw + 8192u);
    dma_piece<0>(c, m0w3, sw3); }
  asm volatile("s_waitcnt vmcnt(0)\n\ts_barrier" ::: "memory");
  const unsigned base = blockIdx.x * 576u;
  run_block<2>(c, x, out, base + 64u * wid, ldsb, wid, lane);
  run_block<2>(c, x, out, base + 256u + 64u * wid, ldsb, wid, lane);
  run_block<1>(c, x, out, base + 512u + 16u * wid, ldsb, wid, lane);
}

extern "C" void kernel_launch(void* const* d_in, const int* in_sizes, int n_in, void* d_out, int out_size, void* d_ws, size_t ws_size, hipStream_t stream) {
  const u16* x = (const u16*)d_in[0]; const u16* wq = (const u16*)d_in[1]; const u16* wkv = (const u16*)d_in[2]; const u16* wo = (const u16*)d_in[3];
  hipFuncSetAttribute((const void*)attn_main, hipFuncAttributeMaxDynamicSharedMemorySize, (int)LDS_BYTES);
  prep_kernel<<<1154, 192, 0, stream>>>(x, wq, wkv, wo, (char*)d_ws);
  attn_main<<<256, 256, LDS_BYTES, stream>>>(x, (const char*)d_ws, (u16*)d_out);
}
```
